```python
import torch

def _softmax(x, axis=-1):
    return torch.softmax(x, dim=axis)

def _gelu(x):
    return torch.nn.functional.gelu(x, approximate='tanh')

def _silu(x):
    return torch.nn.functional.silu(x)

def _sigmoid(x):
    return torch.sigmoid(x)

def _layernorm(x, axis=-1, eps=1e-05):
    m = x.mean(dim=axis, keepdim=True)
    v = x.var(dim=axis, keepdim=True, unbiased=False)
    return (x - m) / torch.sqrt(v + eps)

def _scatter_add(tgt, idx, src, axis=0):
    return tgt.clone().index_add_(axis, idx, src)

def _segment_sum(src, seg, n):
    out = torch.zeros((n,) + src.shape[1:], dtype=src.dtype)
    return out.index_add_(0, seg, src)

def _pad(x, pw):
    flat = tuple((v for pair in reversed(pw) for v in pair))
    return torch.nn.functional.pad(x, flat)
kernel_name = 'parallel_sum__ssm_conv_s4d__mixer_gmlp__prompt'

def setup_inputs():

    _w = lambda a: (lambda t: t.to(torch.bfloat16) if t.is_floating_point() else t)(torch.as_tensor(a))

    def _A_setup():
        Bsz, L, D, N = (1, 2048, 448, 256)
        return {'u': _w((lambda *s: torch.randn(*s, dtype=torch.bfloat16) * 0.05)(Bsz, L, D)), 'log_A_real': _w((lambda *s: torch.randn(*s, dtype=torch.bfloat16) * 12.8)(D, N) * 0.5), 'A_imag': _w((lambda *s: torch.randn(*s, dtype=torch.bfloat16) * 0.1)(D, N) * 3.14), 'C_real': _w((lambda *s: torch.randn(*s, dtype=torch.bfloat16) * 3.2)(D, N) / N ** 0.5), 'C_imag': _w((lambda *s: torch.randn(*s, dtype=torch.bfloat16) * 3.2)(D, N) / N ** 0.5), 'log_dt': _w((lambda *s: torch.randn(*s, dtype=torch.bfloat16) * 0.1)(D) * 0.5 - 4.0), 'D_skip': _w((lambda *s: torch.randn(*s, dtype=torch.bfloat16) * 0.8)(D))}
    _A_in = _A_setup()

    def _B_setup():
        B, S, D, F = (1, 2048, 448, 1792)
        Fh = F // 2
        return {'x': _w((lambda *s: torch.randn(*s, dtype=torch.bfloat16) * 0.05)(B, S, D)), 'w_in': _w((lambda *s: torch.randn(*s, dtype=torch.bfloat16) * 0.4)(D, 2 * Fh)), 'w_sp': _w(0.01 * (lambda *s: torch.randn(*s, dtype=torch.bfloat16) * 0.4)(S, S)), 'b_sp': _w(1.0 + 0.01 * (lambda *s: torch.randn(*s, dtype=torch.bfloat16) * 0.05)(S)), 'w_out': _w((lambda *s: torch.randn(*s, dtype=torch.bfloat16) * 0.05)(Fh, D))}
    _B_in = _B_setup()
    return {'A_u': _A_in['u'], 'A_log_A_real': _A_in['log_A_real'], 'A_A_imag': _A_in['A_imag'], 'A_C_real': _A_in['C_real'], 'A_C_imag': _A_in['C_imag'], 'A_log_dt': _A_in['log_dt'], 'A_D_skip': _A_in['D_skip'], 'B_x': _B_in['x'], 'B_w_in': _B_in['w_in'], 'B_w_sp': _B_in['w_sp'], 'B_b_sp': _B_in['b_sp'], 'B_w_out': _B_in['w_out']}

def reference(A_u, A_log_A_real, A_A_imag, A_C_real, A_C_imag, A_log_dt, A_D_skip, B_x, B_w_in, B_w_sp, B_b_sp, B_w_out):

    def _A_ref(u, log_A_real, A_imag, C_real, C_imag, log_dt, D_skip):
        L = u.shape[1]
        dt = torch.exp(log_dt)
        A = -torch.exp(log_A_real) + 1j * A_imag
        C = (C_real + 1j * C_imag) * dt[:, None]
        pos = (lambda x, dt: x.to(dt))(torch.arange(L), log_dt.dtype)
        P = torch.exp((A * dt[:, None])[:, :, None] * pos[None, None, :])
        K = 2.0 * torch.einsum('dn,dnl->dl', C, P).real
        L2 = 2 * L
        u_t = (lambda x, *d: x.permute(*d))(u, 0, 2, 1)
        Kf = (lambda x, *a, **k: torch.fft.rfft(x.float(), *a, **k))(K, L2)
        Uf = (lambda x, *a, **k: torch.fft.rfft(x.float(), *a, **k))(u_t, L2)
        y = (lambda x, *a, **k: torch.fft.irfft(x, *a, **k).to(torch.bfloat16))(Uf * Kf[None, :, :], L2)[:, :, :L]
        y = (lambda x, *d: x.permute(*d))(y, 0, 2, 1)
        return y + u * D_skip[None, None, :]

    def _B_ref(x, w_in, w_sp, b_sp, w_out):
        B, S, D = x.shape
        F = w_in.shape[-1]
        Fh = F // 2
        z = _gelu(torch.einsum('bsd,df->bsf', x, w_in))
        z1 = z[..., :Fh].clone()
        z2 = _layernorm(z[..., Fh:], axis=-1)
        g = torch.einsum('ts,bsf->btf', w_sp, z2) + b_sp[None, :, None]
        return torch.einsum('bsf,fd->bsd', z1 * g, w_out)
    x = A_u
    xn = _layernorm(x, axis=-1)
    return x + _A_ref(xn, A_log_A_real, A_A_imag, A_C_real, A_C_imag, A_log_dt, A_D_skip) + _B_ref(xn, B_w_in, B_w_sp, B_b_sp, B_w_out)
```

```cpp
#include <hip/hip_runtime.h>
#include <hip/hip_bf16.h>
typedef unsigned short u16;
__device__ __forceinline__ float b2f(u16 v){ return __uint_as_float(((unsigned)v)<<16); }
__device__ __forceinline__ u16 f2b(float f){ __hip_bfloat16 h = __float2bfloat16(f); return *(u16*)&h; }
__device__ __forceinline__ float rb(float f){ return b2f(f2b(f)); }
constexpr int L=2048,D=448,N=256,F=1792,FH=896;
__global__ void k_ln(const u16* x, u16* xn, int ncol, int ldx){
  int row=blockIdx.x; int lane=threadIdx.x; float s=0;
  for(int c=lane;c<ncol;c+=64) s+=b2f(x[row*ldx+c]);
  for(int o=32;o;o>>=1) s+=__shfl_xor(s,o);
  float m=s/ncol; float q=0;
  for(int c=lane;c<ncol;c+=64){ float d=b2f(x[row*ldx+c])-m; q+=d*d; }
  for(int o=32;o;o>>=1) q+=__shfl_xor(q,o);
  float mb=rb(m), vb=rb(q/ncol); float den=rb(sqrtf(rb(vb+1e-5f)));
  for(int c=lane;c<ncol;c+=64){ float d=rb(b2f(x[row*ldx+c])-mb); xn[row*ncol+c]=f2b(d/den); }
}
template<int EPI> __global__ void k_gemm(const u16* A,const u16* B,u16* C,int M,int Nn,int K,const u16* e0,const u16* e1,const u16* e2){
  int n=blockIdx.x*64+threadIdx.x; int m=blockIdx.y; if(n>=Nn) return;
  float acc=0; for(int k=0;k<K;k++) acc+=b2f(A[m*K+k])*b2f(B[k*Nn+n]);
  acc=rb(acc);
  if(EPI==0){ float u=0.7978845608f*(acc+0.044715f*acc*acc*acc); C[m*Nn+n]=f2b(0.5f*acc*(1.f+tanhf(u))); }
  if(EPI==1){ float g=rb(acc+b2f(e0[m])); C[m*Nn+n]=f2b(b2f(e1[m*F+n])*g); }
  if(EPI==2){ float o=rb(b2f(e0[m*Nn+n])+b2f(e1[m*Nn+n])); C[m*Nn+n]=f2b(o+acc); }
}
__global__ void k_K(const u16* lar,const u16* aim,const u16* cr,const u16* ci,const u16* ldt,float* K){
  int d=blockIdx.y; int l=blockIdx.x*64+threadIdx.x; float p=rb((float)l);
  float dt=rb(expf(b2f(ldt[d]))); float acc=0;
  for(int n=0;n<N;n++){ float zr=-rb(expf(b2f(lar[d*N+n])))*dt, zi=b2f(aim[d*N+n])*dt; float e=expf(zr*p); float s,c; sincosf(zi*p,&s,&c);
    acc+= b2f(cr[d*N+n])*dt*e*c - b2f(ci[d*N+n])*dt*e*s; }
  K[d*L+l]=2.f*acc;
}
__global__ void k_conv(const float* K,const u16* xn,const u16* dsk,u16* a){
  int d=blockIdx.x*64+threadIdx.x; int l=blockIdx.y; float acc=0;
  for(int j=0;j<=l;j++) acc+=K[d*L+j]*b2f(xn[(l-j)*D+d]);
  a[l*D+d]=f2b(rb(acc)+rb(b2f(xn[l*D+d])*b2f(dsk[d])));
}
extern "C" void kernel_launch(void* const* d_in, const int* in_sizes, int n_in, void* d_out, int out_size, void* d_ws, size_t ws_size, hipStream_t stream){
  const u16* x=(const u16*)d_in[0];
  char* ws=(char*)d_ws;
  u16* xn=(u16*)ws; u16* z=(u16*)(ws+(4<<20)); u16* z2n=(u16*)(ws+(16<<20)); u16* h=(u16*)(ws+(24<<20)); float* K=(float*)(ws+(32<<20)); u16* a=(u16*)(ws+(40<<20));
  k_ln<<<L,64,0,stream>>>(x,xn,D,D);
  k_gemm<0><<<dim3(F/64,L),64,0,stream>>>(xn,(const u16*)d_in[8],z,L,F,D,0,0,0);
  k_ln<<<L,64,0,stream>>>(z+FH,z2n,FH,F);
  k_gemm<1><<<dim3(FH/64,L),64,0,stream>>>((const u16*)d_in[9],z2n,h,L,FH,L,(const u16*)d_in[10],z,0);
  k_K<<<dim3(L/64,D),64,0,stream>>>((const u16*)d_in[1],(const u16*)d_in[2],(const u16*)d_in[3],(const u16*)d_in[4],(const u16*)d_in[5],K);
  k_conv<<<dim3(D/64,L),64,0,stream>>>(K,xn,(const u16*)d_in[6],a);
  k_gemm<2><<<dim3(D/64,L),64,0,stream>>>(h,(const u16*)d_in[11],(u16*)d_out,L,D,FH,x,a,0);
}
```

```cpp
#include <hip/hip_runtime.h>
#include <hip/hip_bf16.h>
#define ROWMAP_NATURAL 1

typedef unsigned short u16;
typedef unsigned long long u64;
typedef unsigned v4u __attribute__((ext_vector_type(4)));
typedef unsigned v2u __attribute__((ext_vector_type(2)));
typedef float f32x4 __attribute__((ext_vector_type(4)));
typedef float f32x2 __attribute__((ext_vector_type(2)));
typedef short bf16x8 __attribute__((ext_vector_type(8)));
typedef short s16x4 __attribute__((ext_vector_type(4)));
typedef __bf16 bf2 __attribute__((ext_vector_type(2)));
typedef int rsrc4 __attribute__((ext_vector_type(4)));
#define LAS __attribute__((address_space(3)))
#define GAS __attribute__((address_space(1)))
#define RLX __ATOMIC_RELAXED, __HIP_MEMORY_SCOPE_AGENT
#define DI __device__ __forceinline__
#define SU(x) __builtin_amdgcn_readfirstlane(x)
#define SB() __builtin_amdgcn_sched_barrier(0)

constexpr int OFF_G   = 0;
constexpr int OFF_FC  = 4096;
constexpr int OFF_FX  = 8192;
constexpr int OFF_FH  = 131072;
constexpr int OFF_FZ  = 262144;
constexpr int OFF_ST  = 524288;
constexpr int OFF_FK  = 196608;
constexpr int OFF_XNT = 1 << 20;
constexpr int XNT_ROW = 4096;
constexpr int OFF_AT  = 10 << 20;
#define AT_GRAN 1
constexpr int OFF_Z2N = 3 << 20;
#define Z2N_KMAJOR 1
constexpr int OFF_H   = 13 << 19;
#define SINGLE_H 1
constexpr int OFF_KG  = 27 << 19;
constexpr int WS_USED = 15 << 20;
constexpr int OFF_DBG = 24 << 20;

constexpr int LDS_TOTAL = 163840;
constexpr int L_XN = 0;
constexpr int L_WR = 57344;
constexpr int L_SP = 159744;
constexpr int L_SF = 161792;
constexpr int L_TAB = 131072;
constexpr int L_KR = 149504;
constexpr int L_RED = 0;
constexpr int L_UC = 114688;
constexpr int L_CVS = 114688;
constexpr int L_AT = 114688;
constexpr int L_RED3 = 131072;

#define GATHER_SLEEP 12
#define ROT2 0
#define MAP2 1
#define OPOL ""
#define XLATE 0
#define W1E 0
#define SPF 1
#define SPE 0
#define ALATE 0
#define ROT 0
constexpr int PBR = 8;
constexpr int SPIN_CAP = 1 << 16;

DI f32x4 mfma(v4u a, v4u b, f32x4 c) { return __builtin_amdgcn_mfma_f32_16x16x32_bf16(__builtin_bit_cast(bf16x8, a), __builtin_bit_cast(bf16x8, b), c, 0, 0, 0); }
#define GLDS_IMM(vaddr, sbase, LDSC, IMM) \
  asm volatile("s_add_u32 m0, %1, %2\n\ts_nop 0\n\tglobal_load_lds_dwordx4 %0, off offset:%3" ::"v"(vaddr), "s"(sbase), "i"((LDSC) - (IMM)), "i"(IMM) : "memory", "scc")
#define GLDS_SB(voff, sptr, sbase, LDSC) \
  asm volatile("s_add_u32 m0, %2, %3\n\ts_nop 0\n\tglobal_load_lds_dwordx4 %0, %1" ::"v"(voff), "s"(sptr), "s"(sbase), "i"(LDSC) : "memory", "scc")
#define BLD(dst, voff, soff, IMM) asm volatile("buffer_load_dwordx4 %0, %1, %2, %3 offen offset:%4 sc1" : "=v"(dst) : "v"(voff), "s"(rq4), "s"(soff), "i"(IMM) : "memory")
template <int N> DI void wait_vm() { asm volatile("s_waitcnt vmcnt(%0)" ::"i"(N) : "memory"); }
DI void bar() { asm volatile("" ::: "memory"); __builtin_amdgcn_s_waitcnt(0xC07F); __builtin_amdgcn_s_barrier(); asm volatile("" ::: "memory"); }
DI v4u gld16_sc1(const void* p) { v4u r; asm volatile("global_load_dwordx4 %0, %1, off sc1" : "=v"(r) : "v"(p) : "memory"); return r; }
DI unsigned gld4_sc1(const void* sp, unsigned voff) { unsigned r; asm volatile("global_load_dword %0, %1, %2 sc1" : "=v"(r) : "v"(voff), "s"(sp) : "memory"); return r; }
DI unsigned gld2(const void* sp, unsigned voff) { unsigned r; asm volatile("global_load_ushort %0, %1, %2" : "=v"(r) : "v"(voff), "s"(sp) : "memory"); return r; }
DI unsigned pk(float lo, float hi) { f32x2 v = {lo, hi}; return __builtin_bit_cast(unsigned, __builtin_convertvector(v, bf2)); }
DI unsigned pk2(f32x2 v) { return __builtin_bit_cast(unsigned, __builtin_convertvector(v, bf2)); }
DI unsigned f2u(float f) { return __builtin_bit_cast(unsigned, f); }
DI float u2f(unsigned u) { return __builtin_bit_cast(float, u); }
DI float blo(unsigned u) { return u2f(u << 16); }
DI f32x2 up2(unsigned u, unsigned hm) { return f32x2{u2f(u << 16), u2f(u & hm)}; }
DI float rb(float f) { return blo(pk(f, f)); }
DI float dot2(unsigned a, unsigned b, float c) { return __builtin_amdgcn_fdot2_f32_bf16(__builtin_bit_cast(bf2, a), __builtin_bit_cast(bf2, b), c, false); }
template <int CTRL> DI float dpp(float v) { return u2f(__builtin_amdgcn_update_dpp(0u, f2u(v), CTRL, 0xf, 0xf, false)); }
DI f32x2 gelu2(f32x2 a) {
  const float c0 = -2.0f * 0.7978845608028654f * 1.4426950408889634f, c1 = c0 * 0.044715f;
  const f32x2 u = a * (a * a * c1 + c0);
  f32x2 e = {__builtin_amdgcn_exp2f(u.x), __builtin_amdgcn_exp2f(u.y)};
  e = e + 1.0f;
  const f32x2 r = {__builtin_amdgcn_rcpf(e.x), __builtin_amdgcn_rcpf(e.y)};
  return a * r;
}
DI v2u trd(const LAS char* p) { return __builtin_bit_cast(v2u, __builtin_amdgcn_ds_read_tr16_b64_v4i16((LAS s16x4*)p)); }
DI v4u trd2(const LAS char* p, int d) { const v2u lo = trd(p), hi = trd(p + d); return v4u{lo.x, lo.y, hi.x, hi.y}; }
DI void st16_sc1(rsrc4 rq4, int voff, int soff, v4u v) { asm volatile("buffer_store_dwordx4 %0, %1, %2, %3 offen sc1\n\ts_nop 1" ::"v"(v), "v"(voff), "s"(rq4), "s"(soff) : "memory"); }
DI void flag_set(char* ws, int off, unsigned tag) { __hip_atomic_store((GAS unsigned*)(ws + off), tag, RLX); }
DI void poll_words(char* ws, int off, unsigned tag) {
#pragma unroll 1
  for (int sp = 0; sp < SPIN_CAP; ++sp) {
    const unsigned v = __hip_atomic_load((GAS unsigned*)(ws + off), RLX);
    if (__all(v == tag)) break;
    __builtin_amdgcn_s_sleep(1);
  }
  asm volatile("" ::: "memory");
}
#define STAMP(i) do {} while (0)
#define STAMPG(i) do {} while (0)
#define STAMPH(i) do {} while (0)
#define STAMPW(i) do {} while (0)
#define STAMPX(i) do {} while (0)
#define STAMP_DECL do {} while (0)
#define STAMP_OUT do {} while (0)

typedef _Float16 h2 __attribute__((ext_vector_type(2)));
typedef _Float16 h8 __attribute__((ext_vector_type(8)));
DI f32x2 cmul(f32x2 a, f32x2 b) { return f32x2{a.x * b.x - a.y * b.y, a.x * b.y + a.y * b.x}; }
DI f32x2 csq(f32x2 a) { return f32x2{a.x * a.x - a.y * a.y, (a.x + a.x) * a.y}; }
DI h2 toh(f32x2 v) { return __builtin_convertvector(v, h2); }
DI h2 hmul(h2 a, h2 b) { const h2 t = a.xx * b; const h2 bs = {-b.y, b.x}; return __builtin_elementwise_fma(a.yy, bs, t); }
DI f32x2 cexp_p(float zr2, float zrev, float p) {
  const float m = __builtin_amdgcn_exp2f(zr2 * p), f = __builtin_amdgcn_fractf(zrev * p);
  return f32x2{__builtin_amdgcn_cosf(f) * m, __builtin_amdgcn_sinf(f) * m};
}
DI void group8(LAS char* tab, h2 v, h2 S, h2 J, int R0, int R1, int RS) {
#pragma unroll
  for (int k = 0; k < 4; ++k) {
    *(LAS h2*)(tab + (R0 + k * RS) * 288) = v;
    *(LAS h2*)(tab + (R1 + k * RS) * 288) = hmul(v, J);
    if (k < 3) v = hmul(v, S);
  }
}
struct KGen {
  h2 E1, E2, E4, E128, E512, c0, c128, c256, c512, c1024, c2048; float scale; v4u yb[4], xa[4]; f32x4 k0, k1;
  DI void read(const LAS char* tab, int lane, v4u (&d)[4]) {
#pragma unroll
    for (int k = 0; k < 4; ++k) d[k] = *(const LAS v4u*)(tab + (lane & 15) * 288 + (lane >> 4) * 16 + k * 64);
    SB();
  }
  DI f32x4 mul() {
    f32x4 r = {0, 0, 0, 0};
#pragma unroll
    for (int k = 0; k < 4; ++k) r = __builtin_amdgcn_mfma_f32_16x16x32_f16(__builtin_bit_cast(h8, xa[k]), __builtin_bit_cast(h8, yb[k]), r, 0, 0, 0);
    SB();
    return r;
  }
  DI void p1(LAS char* tab, int lane, unsigned p_lar, unsigned p_aim, unsigned p_cr, unsigned p_ci, unsigned p_ldt) {
    const float L2E = 1.4426950408889634f;
    const float dt = rb(__builtin_amdgcn_exp2f(blo(p_ldt) * L2E)), ea = rb(__builtin_amdgcn_exp2f(blo(p_lar) * L2E));
    const float zr2 = -ea * dt * L2E, zrev = blo(p_aim) * dt * 0.15915494309189535f;
    unsigned mx = max(p_cr & 0x7fffu, p_ci & 0x7fffu);
    mx = max(mx, (unsigned)__builtin_amdgcn_update_dpp(0u, mx, 0x128, 0xf, 0xf, false)); mx = max(mx, (unsigned)__builtin_amdgcn_update_dpp(0u, mx, 0x124, 0xf, 0xf, false));
    mx = max(mx, (unsigned)__builtin_amdgcn_update_dpp(0u, mx, 0x122, 0xf, 0xf, false)); mx = max(mx, (unsigned)__builtin_amdgcn_update_dpp(0u, mx, 0x121, 0xf, 0xf, false));
    const unsigned ms = max(max(__builtin_amdgcn_readlane(mx, 0), __builtin_amdgcn_readlane(mx, 16)), max(__builtin_amdgcn_readlane(mx, 32), __builtin_amdgcn_readlane(mx, 48)));
    const unsigned ex = min(max((ms >> 7) + 2u, 2u), 252u);
    scale = u2f(ex << 23) * (2.0f * dt);
    const float inv = u2f((254u - ex) << 23);
    const f32x2 C = {blo(p_cr) * inv, blo(p_ci) * inv};
    const f32x2 e1 = cexp_p(zr2, zrev, 1.0f), y8 = cexp_p(zr2, -zrev, 8.0f), e128 = cexp_p(zr2, zrev, 128.0f);
    const f32x2 y32 = csq(csq(y8)), e2 = csq(e1), e256 = csq(e128), e512 = csq(e256), e1024 = csq(e512), c1k = cmul(C, e1024);
    const h2 Y8 = toh(y8), Y32 = toh(y32);
    LAS char* tw_ = tab + lane * 4;
    group8(tw_, h2{(_Float16)1.0f, (_Float16)0.0f}, Y8, Y32, 0, 4, 1);
    group8(tw_, toh(csq(y32)), Y8, Y32, 8, 12, 1);
    read(tab, lane, yb);
    E1 = toh(e1); E2 = toh(e2); E4 = toh(csq(e2)); E128 = toh(e128); E512 = toh(e512);
    c0 = toh(C); c128 = toh(cmul(C, e128)); c256 = toh(cmul(C, e256)); c512 = toh(cmul(C, e512)); c1024 = toh(c1k); c2048 = toh(cmul(c1k, e1024));
  }
  DI void p2(LAS char* tab, int lane) {
    LAS char* tw_ = tab + lane * 4;
    group8(tw_, c0, E1, E4, 0, 4, 1);
    group8(tw_, c128, E1, E4, 8, 12, 1);
    read(tab, lane, xa);
    group8(tw_, c256, E2, E128, 0, 4, 1);
    group8(tw_, c512, E128, E4, 8, 9, 2);
    SB();
    k0 = mul();
    read(tab, lane, xa);
  }
  DI void p3(LAS char* tab, int lane) {
    const int i = lane & 15, g = lane >> 4;
    LAS char* tw_ = tab + lane * 4;
    group8(tw_, c1024, E128, E512, 0, 4, 1);
    *(LAS h2*)(tw_ + 8 * 288) = c2048;
    SB();
    k1 = mul();
    read(tab, lane, xa);
    LAS float* pc = (LAS float*)tab;
    *(LAS f32x4*)(pc + 128 * (g >> 1) + 4 * (g & 1) + 8 * i) = k0 * scale;
    k1 *= scale;
    if (g < 2) *(LAS f32x4*)(pc + 256 + 64 * g + 4 * i) = k1;
    else { *(LAS f32x2*)(pc + 256 + 64 * g + 2 * i) = k1.xy; *(LAS f32x2*)(pc + 288 + 64 * g + 2 * i) = k1.zw; }
    SB();
    const f32x4 k2 = mul() * scale;
    if (g < 2 || lane == 32) { LAS float* q = pc + 512 + 64 * g + i; q[0] = k2.x; q[16] = k2.y; q[32] = k2.z; q[48] = k2.w; }
  }
};
DI v4u kexpand(const LAS char* TAB, int wn, int lane) {
  if (wn < 2) {
    const LAS float* p = (const LAS float*)TAB + 639 - 64 * wn - lane;
    const float a = (p[0] + p[1152]) + (p[2304] + p[3456]), b = (p[1] + p[1153]) + (p[2305] + p[3457]);
    const unsigned aa = pk(a, a);
    return v4u{pk(b, b), pk(b, (lane & 1) ? a : b), aa, aa};
  } else if (wn == 2) {
    const LAS float* p = (const LAS float*)TAB + 510 - 2 * lane;
    const f32x2 a = (*(const LAS f32x2*)(p) + *(const LAS f32x2*)(p + 1152)) + (*(const LAS f32x2*)(p + 2304) + *(const LAS f32x2*)(p + 3456));
    const float c = (p[2] + p[1154]) + (p[2306] + p[3458]);
    return v4u{pk(c, c), pk(a.y, a.y), pk(a.y, a.x), pk(a.x, a.x)};
  } else {
    const bool e0 = lane >= 32;
    const LAS float* p = (const LAS float*)TAB + (e0 ? 504 - 8 * lane : 380 - 4 * lane);
    const f32x4 a = (*(const LAS f32x4*)(p) + *(const LAS f32x4*)(p + 1152)) + (*(const LAS f32x4*)(p + 2304) + *(const LAS f32x4*)(p + 3456));
    const f32x4 b = (*(const LAS f32x4*)(p + 4) + *(const LAS f32x4*)(p + 1156)) + (*(const LAS f32x4*)(p + 2308) + *(const LAS f32x4*)(p + 3460));
    return v4u{pk(e0 ? b.w : b.x, e0 ? b.z : a.w), pk(e0 ? b.y : a.z, e0 ? b.x : a.z), pk(e0 ? a.w : a.z, e0 ? a.z : a.y), pk(e0 ? a.y : a.x, a.x)};
  }
}
DI void ucopy1(LAS char* p, v4u u0, v4u u1) {
  const unsigned o0 = __builtin_amdgcn_alignbit(u0.y, u0.x, 16), o1 = __builtin_amdgcn_alignbit(u0.z, u0.y, 16), o2 = __builtin_amdgcn_alignbit(u0.w, u0.z, 16),
                 o3 = __builtin_amdgcn_alignbit(u1.x, u0.w, 16), o4 = __builtin_amdgcn_alignbit(u1.y, u1.x, 16);
  *(LAS v4u*)(p) = u0; *(LAS v4u*)(p + 4160) = v4u{o0, o1, o2, o3}; *(LAS v4u*)(p + 8320) = v4u{u0.y, u0.z, u0.w, u1.x}; *(LAS v4u*)(p + 12480) = v4u{o1, o2, o3, o4};
}
DI void ucopies(LAS char* UC, int xt, v4u u0, v4u u1) {
  if (xt == 0) u0 = v4u{0, 0, 0, 0};
  ucopy1(UC + (xt + 1) * 16, u0, u1);
  if (xt == 255) ucopy1(UC + 257 * 16, u1, v4u{0, 0, 0, 0});
  if (xt < 4) *(LAS v4u*)(UC + xt * 4160) = v4u{0, 0, 0, 0};
}
template <int WN> DI void cv_put_(LAS char* S, int lane, f32x4 (&cv)[8]) {
#pragma unroll
  for (int q = 0; q < 8; ++q)
    if ((q >> 1) != WN) *(LAS f32x4*)(S + ((q >> 1) * 6 + (((WN - (q >> 1)) & 3) - 1) * 2 + (q & 1)) * 1024 + lane * 16) = cv[q];
  cv[0] = cv[2 * WN]; cv[1] = cv[2 * WN + 1];
}
DI void cv_put(LAS char* S, int wn, int lane, f32x4 (&cv)[8]) {
  if (wn == 0) cv_put_<0>(S, lane, cv); else if (wn == 1) cv_put_<1>(S, lane, cv); else if (wn == 2) cv_put_<2>(S, lane, cv); else cv_put_<3>(S, lane, cv);
}
DI void cv_out(const LAS char* S, int wn, int lane, const f32x4 (&cv)[8], v2u us0, v2u us1, unsigned p_dsk, unsigned hm, rsrc4 rq4, int ch, unsigned tag) {
  const float ds = blo(p_dsk);
  const LAS char* s = S + wn * 6144 + lane * 16;
  const f32x4 y0 = (cv[0] + *(const LAS f32x4*)(s)) + (*(const LAS f32x4*)(s + 2048) + *(const LAS f32x4*)(s + 4096));
  const f32x4 y1 = (cv[1] + *(const LAS f32x4*)(s + 1024)) + (*(const LAS f32x4*)(s + 3072) + *(const LAS f32x4*)(s + 5120));
  const int vo = 64 * (lane & 15) + 16 * (lane >> 4), so = SU(OFF_AT + ch * 8192 + 2048 * wn);
  st16_sc1(rq4, vo, so, v4u{pk2(up2(us0.x, hm) * ds + y0.xy), tag, pk2(up2(us0.y, hm) * ds + y0.zw), tag});
  st16_sc1(rq4, vo, so + 1024, v4u{pk2(up2(us1.x, hm) * ds + y1.xy), tag, pk2(up2(us1.y, hm) * ds + y1.zw), tag});
}

__global__ __launch_bounds__(512, 2) void fused_kernel(const u16* __restrict__ x, const u16* __restrict__ lar, const u16* __restrict__ aim,
                                                       const u16* __restrict__ cr, const u16* __restrict__ ci, const u16* __restrict__ ldt,
                                                       const u16* __restrict__ dsk, const u16* __restrict__ win, const u16* __restrict__ wsp,
                                                       const u16* __restrict__ bsp, const u16* __restrict__ wout, u16* __restrict__ out, char* ws) {
  extern __shared__ __attribute__((aligned(16))) char smem_[];
  LAS char* L = (LAS char*)smem_;
  const unsigned lb = (unsigned)(size_t)L;
  const int tid = threadIdx.x, lane = tid & 63, w = SU(tid >> 6);
  const int bid = blockIdx.x, xg = bid & 7, r = bid >> 3;
  const int i = lane & 15, g = lane >> 4;
  const int wm = w >> 2, wn = w & 3;
  const int l16 = lane * 16, xt = wn * 64 + lane;
  const rsrc4 rq4 = {(int)(size_t)ws, (int)((size_t)ws >> 32) & 0xffff, WS_USED, 0x00020000};
  unsigned hm = 0xffff0000u;
  asm volatile("" : "+s"(hm));
  STAMP_DECL;
  STAMP(0);

  if (r >= 28) {
    const int sq = r - 28, sp = xg * 4 + sq;
    unsigned gword = gld4_sc1(ws, OFF_G);
    int ch = 224 + sp * 7 + 4 * wm;
    unsigned p0 = lar[ch * 256 + xt], p1 = aim[ch * 256 + xt], p2 = cr[ch * 256 + xt], p3 = ci[ch * 256 + xt], p4 = ldt[ch];
    unsigned wv = 0;
    {
      u64 pc; unsigned back, len;
      asm volatile("s_getpc_b64 %0\nfk_here:\n\ts_mov_b32 %1, fk_here-fk_code_begin\n\ts_mov_b32 %2, fk_code_end-fk_code_begin" : "=s"(pc), "=s"(back), "=s"(len));
      const unsigned o = (unsigned)(sq * 128 + (tid & 127)) * 128u;
      if (w < 2 && o < len) wv = *(const GAS unsigned*)((pc - back + o) & ~(u64)3);
    }
    asm volatile("s_waitcnt vmcnt(5)" : "+v"(gword)::"memory");
    const unsigned tag = SU(gword) + 1u;
    if (tid == 0) flag_set(ws, OFF_FC + sp * 128, tag);
    STAMP(1);
    LAS char* TAB = L + wm * 36864;
#pragma unroll 1
    for (int rd = 0; rd < 4; ++rd) {
      LAS char* tb = TAB + (rd & 1) * 18432;
      if (rd == 3 && wm == 1) { bar(); break; }
      KGen kg;
      kg.p1(tb + wn * 4608, lane, p0, p1, p2, p3, p4);
      if (rd > 0) { wait_vm<0>(); if (lane == 0) flag_set(ws, OFF_FK + (ch - 225) * 128 + wn * 4, tag); }
      const int nc = min(ch + 1, 447);
      p0 = lar[nc * 256 + xt]; p1 = aim[nc * 256 + xt]; p2 = cr[nc * 256 + xt]; p3 = ci[nc * 256 + xt]; p4 = ldt[nc];
      kg.p2(tb + wn * 4608, lane);
      kg.p3(tb + wn * 4608, lane);
      bar();
      st16_sc1(rq4, xt * 16, SU(OFF_KG + (ch - 224) * 4096), kexpand(tb, wn, lane));
      ch += 1;
    }
    wait_vm<0>();
    if (lane == 0) flag_set(ws, OFF_FK + (ch - 225) * 128 + wn * 4, tag);
    {
      const char* ob = (const char*)wout + (size_t)(sq * 196 + w) * 1024 + l16;
      const unsigned od = lb + 81920 + w * 1024;
#pragma unroll 1
      for (int m = w; m < 196; m += 8) { asm volatile("s_mov_b32 m0, %1\n\ts_nop 0\n\tglobal_load_lds_dwordx4 %0, off" ::"v"(ob), "s"(od) : "memory"); ob += 8192; }
    }
    asm volatile("" ::"v"(wv));
    STAMP(2);
    if (sp == 0 && w == 0) {
#pragma unroll
      for (int k = 0; k < 4; ++k) poll_words(ws, OFF_FX + min(k * 64 + lane, 223) * 128, tag);
      poll_words(ws, OFF_FC + (lane & 31) * 128, tag);
      if (lane == 0) flag_set(ws, OFF_G, tag);
    }
    STAMP_OUT;
    return;
  }

  asm volatile("fk_code_begin:");
  const int r7 = r & 3, rp = 8 * (xg >> 1) + (r < 24 ? (r & 7) : 4 * (xg & 1) + r7), ct = r < 24 ? 4 * (xg & 1) + (r >> 3) : 3, gb = rp * 7 + ct;
  v4u xv[7];
  { const char* xs = (const char*)x + (size_t)(64 * rp + 8 * w) * 896;
    const unsigned xo = (lane >> 3) * 896 + (lane & 7) * 16;
#pragma unroll
    for (int j = 0; j < 7; ++j) asm volatile("global_load_dwordx4 %0, %1, %2 offset:%3" : "=v"(xv[j]) : "v"(xo), "s"(xs), "i"(j * 128) : "memory"); }
  unsigned p0, p1, p2, p3, p4, p5;
  unsigned gword = gld4_sc1(ws, OFF_G);
  { const unsigned pi = (unsigned)(gb * 256 + xt) * 2, pc = (unsigned)gb * 2;
    p0 = gld2(lar, pi); p1 = gld2(aim, pi); p2 = gld2(cr, pi); p3 = gld2(ci, pi); p4 = gld2(ldt, pc); p5 = gld2(dsk, pc + wm * 448); }
  unsigned bs[2];
  { const unsigned bo = (unsigned)(64 * rp + 32 * wm + i) * 2; bs[0] = gld2(bsp, bo); bs[1] = gld2(bsp + 16, bo); }
  const unsigned wvo = (unsigned)(g * 3584 + ((i ^ (2 * (g | ((w & 1) << 2)))) << 4));
  const char* wbase = (const char*)win + (size_t)(8 * w) * 3584 + ct * 256;
  const unsigned wdst = lb + L_WR + w * 2048;
#define W_ISSUE(st) do { const char* s0_ = wbase + (size_t)(st) * (64 * 3584); const char* s1_ = s0_ + 4 * 3584; const char* s2_ = s0_ + 1792; const char* s3_ = s1_ + 1792; \
    GLDS_SB(wvo, s2_, wdst, ((st) % 3) * 32768); GLDS_SB(wvo, s3_, wdst, ((st) % 3) * 32768 + 1024); \
    GLDS_SB(wvo, s0_, wdst, ((st) % 3) * 32768 + 16384); GLDS_SB(wvo, s1_, wdst, ((st) % 3) * 32768 + 17408); } while (0)
  __builtin_amdgcn_s_barrier();
  W_ISSUE(0);
  unsigned one2 = 0x3f803f80u;
  asm volatile("" : "+s"(one2));
  asm volatile("s_waitcnt vmcnt(%7)" : "+v"(xv[0]), "+v"(xv[1]), "+v"(xv[2]), "+v"(xv[3]), "+v"(xv[4]), "+v"(xv[5]), "+v"(xv[6]) : "i"(13 + 4 * W1E) : "memory");
  STAMP(1);
  v4u pf[4], pg[5];
  { float s = 0.f, q = 0.f;
#pragma unroll
    for (int j = 0; j < 7; ++j)
#pragma unroll
      for (int k = 0; k < 4; ++k) { s = dot2(one2, xv[j][k], s); q = dot2(xv[j][k], xv[j][k], q); }
    SB(); W_ISSUE(1); SB();
    s += dpp<0xB1>(s); q += dpp<0xB1>(q);
    s += dpp<0x4E>(s); q += dpp<0x4E>(q);
    s += dpp<0x141>(s); q += dpp<0x141>(q);
    const float m = s * (1.0f / 448.0f);
    const float var = fmaxf(q * (1.0f / 448.0f) - m * m, 0.0f);
    const float rd = __builtin_amdgcn_rcpf(rb(__builtin_amdgcn_sqrtf(rb(rb(var) + 1e-5f)))), nb = -rb(m) * rd;
    const int rho = 8 * w + (lane >> 3);
    LAS char* dst = L + L_XN + rho * 896 + (((lane & 7) ^ ((rho >> 1) & 7)) << 4);
#pragma unroll
    for (int j = 0; j < 7; ++j) {
      v4u o;
#pragma unroll
      for (int k = 0; k < 4; ++k) o[k] = pk2(up2(xv[j][k], hm) * rd + nb);
      *(LAS v4u*)(dst + j * 128) = o;
      if (j == 2 && !W1E) { SB(); W_ISSUE(2); SB(); }
      if (j == (W1E ? 3 : 5)) {
        SB();
        const char* s0_ = wbase + (size_t)(3 + r7) * (64 * 3584); const char* s1_ = s0_ + 4 * 3584;
        asm volatile("global_load_dwordx4 %0, %4, %5\n\tglobal_load_dwordx4 %1, %4, %6\n\tglobal_load_dwordx4 %2, %4, %5 offset:1792\n\tglobal_load_dwordx4 %3, %4, %6 offset:1792"
                     : "=&v"(pf[0]), "=&v"(pf[1]), "=&v"(pf[2]), "=&v"(pf[3]) : "v"(wvo), "s"(s0_), "s"(s1_) : "memory");
        SB();
      }
    } }
  asm volatile("s_waitcnt vmcnt(%9)" : "+v"(gword), "+v"(p0), "+v"(p1), "+v"(p2), "+v"(p3), "+v"(p4), "+v"(p5), "+v"(bs[0]), "+v"(bs[1]) : "i"(12 + 5 * SPE) : "memory");
  bar();
  const unsigned tag = SU(gword) + 1u;
  STAMP(2);
  const int wq = i >> 2, wp = i & 3;
  const LAS char* xa0; const LAS char* xa1;
  { const int xsw = (i >> 1) & 7;
    xa0 = L + L_XN + (32 * wm + i) * 896 + ((g ^ xsw) << 4); xa1 = L + L_XN + (32 * wm + i) * 896 + (((4 + g) ^ xsw) << 4); }
  const LAS char* wf[2][2];
  { const int wsg = 2 * (wq | ((g & 1) << 2)), wo = L_WR + (8 * g + wq) * 256 + (wp & 1) * 8;
    wf[0][0] = L + wo + (((4 * wn + (wp >> 1)) ^ wsg) << 4); wf[1][0] = L + wo + (((4 * wn + 2 + (wp >> 1)) ^ wsg) << 4);
    wf[0][1] = wf[0][0] + 65536; wf[1][1] = wf[1][0] + 65536;
    asm volatile("" : "+v"(wf[0][1]), "+v"(wf[1][1])); }
  v4u fa[2][4], fb[2][8];
#define G1_RA(st, n) fa[(st) & 1][n] = *(const LAS v4u*)((((n) >> 1) ? xa1 : xa0) + ((n) & 1) * (16 * 896) + (st) * 128)
#define G1_RB(st, n) fb[(st) & 1][n] = trd2(wf[(n) & 1][((st) % 3) >> 1] + (((st) % 3) & 1) * 32768 + (((n) >> 1) & 1) * 16384 + ((n) >> 2) * 8192, 1024)
#define G1_READ(st) do { _Pragma("unroll") for (int n = 0; n < 4; ++n) { G1_RA(st, n); G1_RB(st, 2 * n); G1_RB(st, 2 * n + 1); } SB(); } while (0)
#define G1_M2(st, n) do { a2[((n) >> 1) & 1][(n) & 1] = mfma(fb[(st) & 1][((n) >> 2) * 4 + ((n) & 1)], fa[(st) & 1][(n) >> 1], a2[((n) >> 1) & 1][(n) & 1]); \
      a1[((n) >> 1) & 1][(n) & 1] = mfma(fb[(st) & 1][((n) >> 2) * 4 + 2 + ((n) & 1)], fa[(st) & 1][(n) >> 1], a1[((n) >> 1) & 1][(n) & 1]); } while (0)
#define W_PIECE(st, k) do { const char* s_ = wbase + (size_t)(st) * (64 * 3584) + ((k) & 1) * (4 * 3584) + ((k) < 2 ? 1792 : 0); \
      GLDS_SB(wvo, s_, wdst, ((st) % 3) * 32768 + ((k) >> 1) * 16384 + ((k) & 1) * 1024); } while (0)
  const char* spb = (const char*)wsp + (size_t)(64 * rp) * 4096;
  const unsigned spd = lb + 90112 + w * 1024;
#define SP_PF(m) do { const int n_ = min(7 * (7 * (m) + w - (w > 4)) + ct, 255); const char* s_ = spb + n_ * 1024; if (!SPE && w != 4) GLDS_SB(l16, s_, spd, 0); } while (0)
#define G1_STEP(st) do { _Pragma("unroll") for (int n = 0; n < 8; ++n) { G1_M2(st, n); SB(); \
      if ((st) < 6) { if ((n & 1) == 0) G1_RA((st) + 1, n >> 1); G1_RB((st) + 1, n); SB(); } \
      if ((st) < 4 && (n & 1)) { if (n == 1) W_PIECE((st) + 3, 0); if (n == 3) W_PIECE((st) + 3, 1); if (n == 5) W_PIECE((st) + 3, 2); if (n == 7) W_PIECE((st) + 3, 3); SB(); } \
      if ((st) == 4 && (n & 1) && n < 7) { SP_PF(n >> 1); SB(); } if ((st) == 5 && (n & 1) && n < 7) { SP_PF(3 + (n >> 1)); SB(); } } } while (0)
  f32x4 a2[2][2], a1[2][2];
#pragma unroll
  for (int t = 0; t < 2; ++t)
#pragma unroll
    for (int j = 0; j < 2; ++j) { a2[t][j] = f32x4{0, 0, 0, 0}; a1[t][j] = f32x4{0, 0, 0, 0}; }
  G1_READ(0);
  { const int r0 = 32 * (w & 1) + 8 * g + wq, chunk = 2 * (w >> 1) + (wp >> 1);
    const LAS char* p = L + L_XN + r0 * 896 + ct * 128 + (wp & 1) * 8;
    const v2u a = trd(p + ((chunk ^ ((r0 >> 1) & 7)) << 4)), b = trd(p + 4 * 896 + ((chunk ^ (((r0 + 4) >> 1) & 7)) << 4));
    st16_sc1(rq4, i * XNT_ROW + g * 16, SU(OFF_XNT + (64 * ct + 16 * (w >> 1)) * XNT_ROW + (64 * rp + 32 * (w & 1)) * 2), v4u{a.x, a.y, b.x, b.y}); }
  wait_vm<8 + 5 * SPE>(); bar(); STAMPH(2); G1_STEP(0); STAMPH(3);
  wait_vm<8 + 5 * SPE>(); bar(); STAMPH(4); G1_STEP(1); STAMPH(5);
  asm volatile("s_waitcnt vmcnt(4)" : "+v"(pf[0]), "+v"(pf[1]), "+v"(pf[2]), "+v"(pf[3])::"memory");
  bar();
  STAMPH(6); G1_STEP(2); STAMPH(7);
  wait_vm<4>(); bar(); STAMPH(8); G1_STEP(3); STAMPH(9);
  wait_vm<4>(); bar(); STAMPH(10); G1_STEP(4); STAMPH(11);
  if (SPE || w == 4) wait_vm<0>(); else wait_vm<3>();
  bar(); STAMPH(12); G1_STEP(5); STAMPH(13); STAMPH(14);
  G1_STEP(6); STAMPH(15);
  STAMP(3);
  auto rs4 = [&](const float (&v)[4]) {
    const auto e0 = __builtin_amdgcn_permlane16_swap(f2u(v[0]), f2u(v[1]), false, false), e1 = __builtin_amdgcn_permlane16_swap(f2u(v[2]), f2u(v[3]), false, false);
    const unsigned e00 = e0[0], e01 = e0[1], e10 = e1[0], e11 = e1[1];
    const auto e2 = __builtin_amdgcn_permlane32_swap(f2u(u2f(e00) + u2f(e01)), f2u(u2f(e10) + u2f(e11)), false, false);
    const unsigned e20 = e2[0], e21 = e2[1];
    return u2f(e20) + u2f(e21);
  };
  const int spo = wn * 512 + (32 * wm + 16 * (g >> 1) + i) * 8 + (g & 1) * 4;
  { float v[4];
#pragma unroll
    for (int t = 0; t < 2; ++t) {
#pragma unroll
      for (int j = 0; j < 2; ++j) { a2[t][j].xy = gelu2(a2[t][j].xy); a2[t][j].zw = gelu2(a2[t][j].zw); }
      const f32x2 s = (a2[t][0].xy + a2[t][0].zw) + (a2[t][1].xy + a2[t][1].zw);
      const f32x2 q = a2[t][0].xy * a2[t][0].xy + (a2[t][0].zw * a2[t][0].zw + (a2[t][1].xy * a2[t][1].xy + a2[t][1].zw * a2[t][1].zw));
      v[2 * t] = s.x + s.y; v[2 * t + 1] = q.x + q.y;
    }
    *(LAS float*)(L + L_SP + spo) = rs4(v); }
  STAMPW(2);
  bar();
  if (w == 5) {
    f32x2 s = *(const LAS f32x2*)(L + L_SP + lane * 8);
#pragma unroll
    for (int k = 1; k < 4; ++k) s += *(const LAS f32x2*)(L + L_SP + k * 512 + lane * 8);
    st16_sc1(rq4, l16, OFF_ST + gb * 1024, v4u{f2u(s.x), tag, f2u(s.y), tag});
  }
  STAMP(4); STAMPW(3);
  const char* wrow = (const char*)wsp + (size_t)(64 * rp) * 4096;
#define KQ(s) ((((s) >> 2) + ROT * xg) & 7)
  const unsigned avo = (unsigned)((8 * w + (lane >> 3)) * 4096 + (((lane & 7) ^ (4 * (w & 1) + (lane >> 4))) << 4));
  const unsigned adst = lb + w * 2048, adst1 = lb + w * 1024;
#define GLDS_A(voff, sptr, sdst, LDSC, IMM) asm volatile("s_add_u32 m0, %2, %3\n\ts_nop 0\n\tglobal_load_lds_dwordx4 %0, %1 offset:%4" ::"v"(voff), "s"(sptr), "s"(sdst), \
                               "i"((LDSC) - (IMM)), "i"(IMM) : "memory", "scc")
  const int aj = w - 5;
  const unsigned av0 = (unsigned)((8 * aj + (lane >> 3)) * 4096 + (((lane & 7) ^ (4 * (aj & 1) + (lane >> 4))) << 4)), av1 = (av0 ^ 64) + 24 * 4096, av2 = av0 + 48 * 4096;
  const unsigned ad3 = lb + aj * 1024;
#define KS(s) (KQ(s) * 4 + (((s) + ROT2 * r7) & 3))
#define WQ(s) (wrow + KS(s) * 128)
#define A3P(s, n) GLDS_A((n) == 0 ? av0 : (n) == 1 ? av1 : av2, WQ(s), ad3, (s) * 8192 + (n) * 3072, 0)
#define A3X(s) do { A3P(s, 0); A3P(s, 1); if (aj < 2) A3P(s, 2); } while (0)
  v4u z1f[2];
  auto z1_out = [&]() {
#pragma unroll
    for (int t = 0; t < 2; ++t) z1f[t] = v4u{pk2(gelu2(a1[t][0].xy)), pk2(gelu2(a1[t][0].zw)), pk2(gelu2(a1[t][1].xy)), pk2(gelu2(a1[t][1].zw))};
    asm volatile("" : "+v"(z1f[0]), "+v"(z1f[1]));
  };
  v4u sg_[7];
  const char* stp = ws + OFF_ST + rp * 7168 + l16;
  KGen kg;
  if (wm == 0) { z1_out(); STAMPW(4); kg.p1(L + L_TAB + wn * 4608, lane, p0, p1, p2, p3, p4); }
  else if (w != 4 && !ALATE) { A3X(0); A3X(1); A3X(2); A3X(3); }
  STAMPW(5);
  if (w == 4) {
    __builtin_amdgcn_s_setprio(3);
    __builtin_amdgcn_s_sleep(GATHER_SLEEP);
#pragma unroll
    for (int k = 0; k < 7; ++k) sg_[k] = gld16_sc1(stp + k * 1024);
    asm volatile("s_waitcnt vmcnt(0)" : "+v"(sg_[0]), "+v"(sg_[1]), "+v"(sg_[2]), "+v"(sg_[3]), "+v"(sg_[4]), "+v"(sg_[5]), "+v"(sg_[6])::"memory");
    STAMPW(6);
#pragma unroll 1
    for (int sp = 0; sp < SPIN_CAP; ++sp) {
      unsigned df = 0;
#pragma unroll
      for (int k = 0; k < 7; ++k) df |= (sg_[k].y ^ tag) | (sg_[k].w ^ tag);
      if (__all(df == 0)) break;
#pragma unroll
      for (int k = 0; k < 7; ++k) sg_[k] = gld16_sc1(stp + k * 1024);
      asm volatile("s_waitcnt vmcnt(0)" : "+v"(sg_[0]), "+v"(sg_[1]), "+v"(sg_[2]), "+v"(sg_[3]), "+v"(sg_[4]), "+v"(sg_[5]), "+v"(sg_[6])::"memory");
    }
    STAMPW(7);
    float s = 0.f, q = 0.f;
#pragma unroll
    for (int k = 0; k < 7; ++k) { s += u2f(sg_[k].x); q += u2f(sg_[k].z); }
    const float m = s * (1.0f / 896.0f);
    const float var = fmaxf(q * (1.0f / 896.0f) - m * m, 0.0f);
    const float rd = __builtin_amdgcn_rcpf(rb(__builtin_amdgcn_sqrtf(rb(rb(var) + 1e-5f))));
    *(LAS f32x2*)(L + L_SF + lane * 8) = f32x2{rd, -rb(m) * rd};
    __builtin_amdgcn_s_setprio(0);
  }
  STAMPW(8);
  bar();
  STAMP(5); STAMPW(9);
  { LAS char* sc = L + (wm ? L_KR + wn * 2048 : L_TAB + wn * 4608);
#pragma unroll
    for (int t = 0; t < 2; ++t) {
      const f32x2 st = *(const LAS f32x2*)(L + L_SF + (32 * wm + 16 * t + i) * 8);
#pragma unroll
      for (int j = 0; j < 2; ++j)
        *(LAS v2u*)(sc + (16 * t + i) * 64 + ((j ^ (i >> 3)) << 5) + g * 8) = v2u{pk2(a2[t][j].xy * st.x + st.y), pk2(a2[t][j].zw * st.x + st.y)};
    }
    const int so = SU(OFF_Z2N + ((2 * rp + wm) * 56 + 8 * ct + 2 * wn) * 1024);
    const LAS char* p = sc + (8 * g + wq) * 64 + wp * 8;
    st16_sc1(rq4, l16, so, trd2(p + ((g & 1) << 5), 256));
    st16_sc1(rq4, l16, so + 1024, trd2(p + (((g & 1) ^ 1) << 5), 256)); }
  SB(); STAMPW(10);
  if (wm == 0) kg.p2(L + L_TAB + wn * 4608, lane);
  wait_vm<0>();
  STAMPW(11);
  bar();
  if (w == 4 && lane < 33) flag_set(ws, lane < 32 ? OFF_FZ + (lane * 7 + ct) * 1024 + (rp >> 2) * 128 + (rp & 3) * 4 : OFF_FX + (ct * 32 + rp) * 128, tag);
  STAMP(6); STAMPW(12);
  if (wm == 0) kg.p3(L + L_TAB + wn * 4608, lane);
  else {
    if (w != 4 && ALATE) { A3X(0); A3X(1); A3X(2); A3X(3); }
    SB(); z1_out(); SB();
    if (w != 4) { A3X(4); A3X(5); A3X(6); A3X(7); A3X(8); A3X(9); }
  }
  STAMP(7); STAMPW(13);
  bar();
  if (wm == 0) {
    const int fzo = OFF_FZ + gb * 1024 + ((lane & 31) >> 2) * 128 + wp * 4;
    unsigned fz0 = gld4_sc1(ws, fzo);
    *(LAS v4u*)(L + L_KR + xt * 16) = kexpand(L + L_TAB, wn, lane);
    if (xt < 64) *(LAS v4u*)(L + L_KR + 4096 + (xt >> 5) * 4608 + (xt & 31) * 16) = v4u{0, 0, 0, 0};
    asm volatile("s_waitcnt vmcnt(0)" : "+v"(fz0)::"memory");
    if (w == 0 && !__all(fz0 == tag)) poll_words(ws, fzo, tag);
  } else if (w != 4) { A3X(10); A3X(11); A3X(12); A3X(13); A3X(14); A3X(15); wait_vm<12>(); }
  STAMPW(14);
  bar();
  STAMPW(15);
  STAMP(8);
  constexpr int PB = PBR;
  v4u bz[PB][2];
  const int zso = SU(OFF_Z2N + (wm * 56 + 8 * ct + 2 * wn) * 1024);
#define B_LOAD(s) do { B_L0(s); B_L1(s); } while (0)
#define B_L0(s) do { const int so_ = zso + KS(s) * 114688; BLD(bz[(s) % PB][0], l16, so_, 0); } while (0)
#define B_L1(s) do { const int so_ = zso + KS(s) * 114688; BLD(bz[(s) % PB][1], l16, so_, 1024); } while (0)
  B_LOAD(0); B_LOAD(1); B_LOAD(2);
  const char* os0; const char* os1;
  { const int rho = 16 * w + (lane >> 3);
    os0 = (const char*)wout + (size_t)rho * 896 + ct * 128 + (((lane & 7) ^ (2 * ((rho >> 1) & 3))) << 4); os1 = os0 + 8 * 896; }
  f32x4 acc[2][4];
#pragma unroll
  for (int t = 0; t < 4; ++t) { const float bb = t < 2 ? blo(bs[t & 1]) : 0.f; acc[0][t] = f32x4{bb, bb, bb, bb}; acc[1][t] = acc[0][t]; }
  unsigned fx = 0;
  v4u u0, u1;
  const int ch = gb + 224 * wm;
  const int fxo = (wm && (lane & 32)) ? OFF_FK + gb * 128 + (lane & 3) * 4 : OFF_FX + ((ch >> 6) * 32 + (lane & 31)) * 128;
  { const LAS char* ak[3]; const LAS char* as[3];
    { const LAS char* ab = L + i * 128 + (((4 * wm + g) ^ ((i >> 1) & 7)) << 4);
      ak[0] = ab + wm * 4096; as[0] = ab + (wm ^ 1) * 4096;
      ak[1] = ak[0] + 65536; as[1] = as[0] + 65536; ak[2] = ak[0] + 131072; as[2] = as[0] + 131072;
      asm volatile("" : "+v"(ak[1]), "+v"(as[1]), "+v"(ak[2]), "+v"(as[2])); }
    v4u aA[4], aB[4];
#define A_RD1(dst, s, t) dst[t] = *(const LAS v4u*)(((t) < 2 ? ak : as)[((s) % 18) >> 3] + ((s) % 18 & 7) * 8192 + ((t) & 1) * 2048)
#define A_READ(dst, s) do { _Pragma("unroll") for (int t = 0; t < 4; ++t) A_RD1(dst, s, t); SB(); } while (0)
#define B_WAIT(s, n) asm volatile("s_waitcnt vmcnt(%2)" : "+v"(bz[(s) % PB][0]), "+v"(bz[(s) % PB][1]) : "i"(n) : "memory")
#define G2_M(cur, nxt, s, t) do { acc[0][t] = mfma(bz[(s) % PB][0], cur[t], acc[0][t]); acc[1][t] = mfma(bz[(s) % PB][1], cur[t], acc[1][t]); SB(); \
      if ((s) < 31) { A_RD1(nxt, (s) + 1, t); SB(); } } while (0)
#define A_P0(s) GLDS_A(avo, WQ(s), adst1, ((s) % 18) * 8192, 0)
#define U_CHECK() do { fx = gld4_sc1(ws, fxo); } while (0)
#define RED_W(t) do { *(LAS f32x4*)(L + L_RED + w * 4096 + l16 + ((t) - 2) * 1024) = acc[0][t]; *(LAS f32x4*)(L + L_RED + w * 4096 + l16 + (t) * 1024) = acc[1][t]; SB(); } while (0)
#define U_OK(n) do { asm volatile("s_waitcnt vmcnt(%1)" : "+v"(fx) : "i"(n) : "memory"); if (!__all(fx == tag)) poll_words(ws, fxo, tag); } while (0)
#define U_L0() do { const int so_ = SU(OFF_XNT + ch * 4096), v_ = (xt > 0 ? xt - 1 : 0) * 16; BLD(u0, v_, so_, 0); } while (0)
#define U_L1() do { const int so_ = SU(OFF_XNT + ch * 4096), v_ = xt * 16; BLD(u1, v_, so_, 0); } while (0)
#define O_P(n) do { const char* q_ = (((n) & 1) ? os1 : os0) + (size_t)((n) >> 1) * (128 * 896); GLDS_IMM(q_, adst, ((n) >> 1) * 16384 + ((n) & 1) * 1024, 0); } while (0)
#define O_PF(m) do { if (!SPF && ((m) < 3 || w < 4)) { const char* s_ = (const char*)wout + (size_t)(8 * (4 * (8 * (m) + w) + r7)) * 896 + ct * 128; GLDS_SB(ovo, s_, adst, L_UC); } } while (0)
    const unsigned ovo = (unsigned)((lane >> 3) * 896 + (lane & 7) * 16);
#define K_P() do { if (wm) { const char* ks_ = ws + OFF_KG + gb * 4096 + xt * 16; const unsigned kd_ = lb + L_KR + 4608 + wn * 1024; \
        asm volatile("s_mov_b32 m0, %1\n\ts_nop 0\n\tglobal_load_lds_dwordx4 %0, off sc1" ::"v"(ks_), "s"(kd_) : "memory"); } } while (0)
#define PRIO(n) __builtin_amdgcn_s_setprio(n)
    A_READ(aA, 0);
    B_WAIT(0, 4); PRIO(3); G2_M(aA, aB, 0, 0); B_L0(3); SB(); G2_M(aA, aB, 0, 1); B_L1(3); SB(); G2_M(aA, aB, 0, 2); B_L0(4); SB(); G2_M(aA, aB, 0, 3); B_L1(4); SB();
    B_WAIT(1, 6); PRIO(3); G2_M(aB, aA, 1, 0); B_L0(5); SB(); G2_M(aB, aA, 1, 1); B_L1(5); SB(); G2_M(aB, aA, 1, 2); B_L0(6); SB(); G2_M(aB, aA, 1, 3); B_L1(6); SB(); STAMPG(2);
    B_WAIT(2, 8); PRIO(2); G2_M(aA, aB, 2, 0); B_L0(7); SB(); G2_M(aA, aB, 2, 1); B_L1(7); SB(); G2_M(aA, aB, 2, 2); B_L0(8); SB(); G2_M(aA, aB, 2, 3); B_L1(8); SB();
    B_WAIT(3, 10); PRIO(2); G2_M(aB, aA, 3, 0); B_L0(9); SB(); G2_M(aB, aA, 3, 1); B_L1(9); SB(); G2_M(aB, aA, 3, 2); A_P0(16); SB(); G2_M(aB, aA, 3, 3); STAMPG(3);
    B_WAIT(4, 11); PRIO(1); G2_M(aA, aB, 4, 0); B_L0(10); SB(); G2_M(aA, aB, 4, 1); B_L1(10); SB(); G2_M(aA, aB, 4, 2); A_P0(17); SB(); G2_M(aA, aB, 4, 3);
    B_WAIT(5, 12); PRIO(1); G2_M(aB, aA, 5, 0); B_L0(11); SB(); G2_M(aB, aA, 5, 1); B_L1(11); SB(); G2_M(aB, aA, 5, 2); G2_M(aB, aA, 5, 3); STAMPG(4);
    wait_vm<26>(); bar(); B_WAIT(6, 12); PRIO(3); G2_M(aA, aB, 6, 0); B_L0(12); SB(); G2_M(aA, aB, 6, 1); B_L1(12); SB(); G2_M(aA, aB, 6, 2); A_P0(18); SB(); G2_M(aA, aB, 6, 3);
    B_WAIT(7, 13); PRIO(3); G2_M(aB, aA, 7, 0); B_L0(13); SB(); G2_M(aB, aA, 7, 1); B_L1(13); SB(); G2_M(aB, aA, 7, 2); A_P0(19); SB(); G2_M(aB, aA, 7, 3); STAMPG(5);
    B_WAIT(8, 14); PRIO(2); G2_M(aA, aB, 8, 0); B_L0(14); SB(); G2_M(aA, aB, 8, 1); B_L1(14); SB(); G2_M(aA, aB, 8, 2); A_P0(20); SB(); G2_M(aA, aB, 8, 3);
    B_WAIT(9, 15); PRIO(2); G2_M(aB, aA, 9, 0); B_L0(15); SB(); G2_M(aB, aA, 9, 1); B_L1(15); SB(); G2_M(aB, aA, 9, 2); A_P0(21); SB(); G2_M(aB, aA, 9, 3); STAMPG(6);
    B_WAIT(10, 15); PRIO(1); G2_M(aA, aB, 10, 0); B_L0(16); SB(); G2_M(aA, aB, 10, 1); B_L1(16); SB(); G2_M(aA, aB, 10, 2); A_P0(22); SB(); G2_M(aA, aB, 10, 3);
    B_WAIT(11, 15); PRIO(1); G2_M(aB, aA, 11, 0); B_L0(17); SB(); G2_M(aB, aA, 11, 1); B_L1(17); SB(); G2_M(aB, aA, 11, 2); A_P0(23); SB(); G2_M(aB, aA, 11, 3); STAMPG(7);
    wait_vm<15>(); bar(); B_WAIT(12, 16); PRIO(3); G2_M(aA, aB, 12, 0); B_L0(18); SB(); G2_M(aA, aB, 12, 1); B_L1(18); SB(); G2_M(aA, aB, 12, 2); A_P0(24); SB(); G2_M(aA, aB, 12, 3);
    B_WAIT(13, 16); PRIO(3); G2_M(aB, aA, 13, 0); B_L0(19); SB(); G2_M(aB, aA, 13, 1); B_L1(19); SB(); G2_M(aB, aA, 13, 2); A_P0(25); SB(); G2_M(aB, aA, 13, 3); STAMPG(8);
    B_WAIT(14, 16); PRIO(2); G2_M(aA, aB, 14, 0); B_L0(20); SB(); G2_M(aA, aB, 14, 1); B_L1(20); SB(); G2_M(aA, aB, 14, 2); A_P0(26); SB(); G2_M(aA, aB, 14, 3);
    B_WAIT(15, 16); PRIO(2); G2_M(aB, aA, 15, 0); B_L0(21); SB(); G2_M(aB, aA, 15, 1); B_L1(21); SB(); G2_M(aB, aA, 15, 2); A_P0(27); SB(); G2_M(aB, aA, 15, 3); STAMPG(9);
    B_WAIT(16, 16); PRIO(1); G2_M(aA, aB, 16, 0); B_L0(22); SB(); G2_M(aA, aB, 16, 1); B_L1(22); SB(); G2_M(aA, aB, 16, 2); A_P0(28); SB(); G2_M(aA, aB, 16, 3);
    B_WAIT(17, 16); PRIO(1); G2_M(aB, aA, 17, 0); B_L0(23); SB(); G2_M(aB, aA, 17, 1); B_L1(23); SB(); G2_M(aB, aA, 17, 2); A_P0(29); SB(); G2_M(aB, aA, 17, 3); STAMPG(10);
    wait_vm<12>(); bar(); B_WAIT(18, 16); PRIO(3); G2_M(aA, aB, 18, 0); B_L0(24); SB(); G2_M(aA, aB, 18, 1); B_L1(24); SB(); G2_M(aA, aB, 18, 2); A_P0(30); SB(); G2_M(aA, aB, 18, 3);
    B_WAIT(19, 16); PRIO(3); G2_M(aB, aA, 19, 0); B_L0(25); SB(); G2_M(aB, aA, 19, 1); B_L1(25); SB(); G2_M(aB, aA, 19, 2); A_P0(31); SB(); G2_M(aB, aA, 19, 3); STAMPG(11);
    B_WAIT(20, 16); PRIO(2); G2_M(aA, aB, 20, 0); B_L0(26); SB(); G2_M(aA, aB, 20, 1); B_L1(26); SB(); G2_M(aA, aB, 20, 2); U_CHECK(); SB(); G2_M(aA, aB, 20, 3);
    B_WAIT(21, 16); PRIO(2); G2_M(aB, aA, 21, 0); B_L0(27); SB(); G2_M(aB, aA, 21, 1); B_L1(27); SB(); G2_M(aB, aA, 21, 2); O_PF(0); SB(); G2_M(aB, aA, 21, 3); STAMPG(12);
    B_WAIT(22, 15); PRIO(1); G2_M(aA, aB, 22, 0); B_L0(28); SB(); G2_M(aA, aB, 22, 1); B_L1(28); SB(); G2_M(aA, aB, 22, 2); O_PF(1); SB(); G2_M(aA, aB, 22, 3);
    B_WAIT(23, 14); PRIO(1); G2_M(aB, aA, 23, 0); B_L0(29); SB(); G2_M(aB, aA, 23, 1); B_L1(29); SB(); G2_M(aB, aA, 23, 2); O_PF(2); SB(); G2_M(aB, aA, 23, 3); STAMPG(13);
    B_WAIT(24, 13); PRIO(0); G2_M(aA, aB, 24, 0); B_L0(30); SB(); G2_M(aA, aB, 24, 1); B_L1(30); SB(); G2_M(aA, aB, 24, 2); O_PF(3); SB(); G2_M(aA, aB, 24, 3);
    wait_vm<11>(); bar(); B_WAIT(25, 12); U_OK(8); PRIO(3); G2_M(aB, aA, 25, 0); B_L0(31); SB(); G2_M(aB, aA, 25, 1); B_L1(31); SB(); G2_M(aB, aA, 25, 2); U_L0(); SB(); G2_M(aB, aA, 25, 3); U_L1(); SB(); STAMPG(14);
    B_WAIT(26, 13); PRIO(3); G2_M(aA, aB, 26, 0); K_P(); SB(); G2_M(aA, aB, 26, 1); O_P(4); SB(); G2_M(aA, aB, 26, 2); G2_M(aA, aB, 26, 3);
    B_WAIT(27, 11); PRIO(2); G2_M(aB, aA, 27, 0); O_P(5); SB(); G2_M(aB, aA, 27, 1); G2_M(aB, aA, 27, 2); G2_M(aB, aA, 27, 3); STAMPG(15);
    B_WAIT(28, 10); PRIO(2); G2_M(aA, aB, 28, 0); O_P(6); SB(); G2_M(aA, aB, 28, 1); G2_M(aA, aB, 28, 2); G2_M(aA, aB, 28, 3);
    B_WAIT(29, 9); PRIO(1); G2_M(aB, aA, 29, 0); O_P(7); SB(); G2_M(aB, aA, 29, 1); G2_M(aB, aA, 29, 2); G2_M(aB, aA, 29, 3); STAMPG(15);
    B_WAIT(30, 8); PRIO(1); G2_M(aA, aB, 30, 0); G2_M(aA, aB, 30, 1); G2_M(aA, aB, 30, 2); G2_M(aA, aB, 30, 3);
    bar(); B_WAIT(31, 6); PRIO(3); G2_M(aB, aA, 31, 2); G2_M(aB, aA, 31, 3); G2_M(aB, aA, 31, 0); RED_W(2); G2_M(aB, aA, 31, 1); RED_W(3); STAMPG(15);
  }
  PRIO(0);
  STAMP(9);
  bar();
  { const int so = SU(OFF_H + ((4 * rp + 2 * wm) * 28 + 4 * ct + wn) * 1024);
    const LAS char* rp_ = L + L_RED + (w ^ 4) * 4096 + l16;
#pragma unroll
    for (int t = 0; t < 2; ++t) {
      v4u o;
#pragma unroll
      for (int j = 0; j < 2; ++j) {
        const f32x4 v = acc[j][t] + *(const LAS f32x4*)(rp_ + (j * 2 + t) * 1024);
        o[2 * j] = pk2(up2(z1f[t][2 * j], hm) * v.xy); o[2 * j + 1] = pk2(up2(z1f[t][2 * j + 1], hm) * v.zw);
      }
      st16_sc1(rq4, l16, so + t * 28 * 1024, o);
    } }
  STAMP(10);
  asm volatile("s_waitcnt vmcnt(2)" : "+v"(u0), "+v"(u1)::"memory");
  ucopies(L + L_UC + wm * 16640, xt, u0, u1);
  STAMPX(3);
  wait_vm<0>();
  STAMPX(4);
  bar();
  if (tid < 7) flag_set(ws, OFF_FH + (rp * 7 + tid) * 128 + ct * 4, tag);
  STAMP(11);
  const unsigned xo = (unsigned)((64 * rp + 16 * wn + i) * 448 + 64 * ct + 32 * wm + 4 * g) * 2;
  v2u xr0, xr1;
  asm volatile("global_load_dwordx2 %0, %2, %3\n\tglobal_load_dwordx2 %1, %2, %3 offset:32" : "=&v"(xr0), "=&v"(xr1) : "v"(xo), "s"(x) : "memory");
  f32x4 cv[8]; v2u us0, us1;
  const int fho = OFF_FH + gb * 128 + min(lane & 7, 6) * 4;
  unsigned fh;
  v4u hf[14];
  const int hso = SU(OFF_H + ((4 * rp + wn) * 28 + 14 * wm) * 1024);
#define H_LOAD(n) do { const int so_ = hso + ((n) >> 2) * 4096; BLD(hf[n], l16, so_, ((n) & 3) * 1024); } while (0)
  { const LAS char* UC = L + L_UC + wm * 16640;
#pragma unroll
    for (int q = 0; q < 8; ++q) cv[q] = f32x4{0, 0, 0, 0};
    const LAS char* al = UC + ((i + 1) & 3) * 4160 + ((i + 1) >> 2) * 8 + 16 * g + 128 * wn;
    const LAS char* ah = al + 8;
    asm volatile("" : "+v"(ah));
    const LAS char* bp = L + L_KR + wm * 4608 + (240 + 64 * wn) * 2 - 32 * i + 16 * g;
    v4u ca[2][8], cb[2][8];
#define CV_A(ph, t) do { const v2u lo_ = *(const LAS v2u*)(al + (ph) * 64 + (t) * 512), hi_ = *(const LAS v2u*)(ah + (ph) * 64 + (t) * 512); ca[ph][t] = v4u{lo_.x, lo_.y, hi_.x, hi_.y}; SB(); } while (0)
#define CV_B(ph, e) do { cb[ph][e] = *(const LAS v4u*)(bp + (ph) * 64 + (7 - (e)) * 512); SB(); } while (0)
#define CV_M(ph, t, q) do { cv[q] = mfma(ca[ph][t], cb[ph][(q) - (t)], cv[q]); SB(); } while (0)
#define FH_LD() do { fh = gld4_sc1(ws, fho); SB(); } while (0)
#define FH_OK() do { asm volatile("s_waitcnt vmcnt(0)" : "+v"(fh)::"memory"); if (!__all(fh == tag)) poll_words(ws, fho, tag); } while (0)
    CV_A(0, 0); CV_B(0, 0); CV_B(0, 1); CV_B(0, 2); CV_B(0, 3); CV_B(0, 4); CV_B(0, 5); CV_B(0, 6); CV_B(0, 7); CV_A(0, 1);
    CV_M(0, 0, 0); CV_A(0, 2); CV_M(0, 0, 1); CV_A(0, 3); O_P(0); SB(); CV_M(0, 0, 2); CV_A(0, 4); CV_M(0, 0, 3); CV_A(0, 5); CV_M(0, 0, 4); CV_A(0, 6); O_P(1); SB(); CV_M(0, 0, 5); CV_A(0, 7); CV_M(0, 0, 6); CV_A(1, 0); CV_M(0, 0, 7); O_P(8); SB();
    CV_M(0, 1, 1); CV_B(1, 0); CV_M(0, 1, 2); CV_M(0, 1, 3); CV_B(1, 1); O_P(9); SB(); CV_M(0, 1, 4); CV_M(0, 1, 5); CV_B(1, 2); CV_M(0, 1, 6); O_P(2); SB(); CV_M(0, 1, 7); CV_B(1, 3);
    CV_M(0, 2, 2); CV_M(0, 2, 3); CV_B(1, 4); O_P(3); SB(); CV_M(0, 2, 4); CV_M(0, 2, 5); CV_B(1, 5); CV_M(0, 2, 6); O_P(10); SB(); CV_M(0, 2, 7); CV_B(1, 6);
    CV_M(0, 3, 3); CV_M(0, 3, 4); CV_B(1, 7); O_P(11); SB(); CV_M(0, 3, 5); CV_M(0, 3, 6); CV_A(1, 1); CV_M(0, 3, 7); O_P(12); SB();
    CV_M(0, 4, 4); CV_A(1, 2); CV_M(0, 4, 5); CV_M(0, 4, 6); CV_A(1, 3); O_P(13); SB(); CV_M(0, 4, 7);
    CV_M(0, 5, 5); CV_A(1, 4); CV_M(0, 5, 6); CV_M(0, 5, 7); CV_A(1, 5);
    CV_M(0, 6, 6); CV_M(0, 6, 7); CV_A(1, 6);
    CV_M(0, 7, 7);
    CV_M(1, 0, 0); CV_A(1, 7); CV_M(1, 0, 1); CV_M(1, 0, 2); FH_LD(); CV_M(1, 0, 3); CV_M(1, 0, 4); CV_M(1, 0, 5); CV_M(1, 0, 6); CV_M(1, 0, 7);
    CV_M(1, 1, 1); CV_M(1, 1, 2); CV_M(1, 1, 3); CV_M(1, 1, 4); CV_M(1, 1, 5); CV_M(1, 1, 6); CV_M(1, 1, 7); FH_OK();
    CV_M(1, 2, 2); H_LOAD(0); SB(); CV_M(1, 2, 3); CV_M(1, 2, 4); CV_M(1, 2, 5); CV_M(1, 2, 6); H_LOAD(1); SB(); CV_M(1, 2, 7);
    CV_M(1, 3, 3); CV_M(1, 3, 4); CV_M(1, 3, 5); H_LOAD(2); SB(); CV_M(1, 3, 6); CV_M(1, 3, 7);
    CV_M(1, 4, 4); CV_M(1, 4, 5); H_LOAD(3); SB(); CV_M(1, 4, 6); CV_M(1, 4, 7);
    CV_M(1, 5, 5); CV_M(1, 5, 6); H_LOAD(4); SB(); CV_M(1, 5, 7);
    CV_M(1, 6, 6); CV_M(1, 6, 7);
    CV_M(1, 7, 7);
    const LAS char* up = UC + 32 + (512 * wn + 16 * i + 4 * g) * 2;
    us0 = *(const LAS v2u*)(up); us1 = *(const LAS v2u*)(up + 512); }
  STAMPX(6);
  STAMPX(7);
  bar();
  STAMP(12);
  H_LOAD(5); H_LOAD(6); H_LOAD(7);
  cv_put(L + L_CVS + wm * 24576, wn, lane, cv);
  H_LOAD(8); H_LOAD(9); H_LOAD(10);
  bar();
  STAMPX(9);
  cv_out(L + L_CVS + wm * 24576, wn, lane, cv, us0, us1, p5, hm, rq4, ch, tag);
  STAMP(13);
  f32x4 a3[4];
#pragma unroll
  for (int c = 0; c < 4; ++c) a3[c] = f32x4{0, 0, 0, 0};
  {
    const int rsw = 2 * (((4 * g + wq) >> 1) & 3);
    const LAS char* wb = L + (4 * g + wq) * 128 + (wp & 1) * 8;
    const LAS char* wc[4];
#pragma unroll
    for (int c = 0; c < 4; ++c) wc[c] = wb + wm * 57344 + (((2 * (c ^ (2 * wm)) + (wp >> 1)) ^ rsw) << 4);
    v4u wfr[3][4], av0, av1;
    const int aso = SU(OFF_AT + (64 * ct + 8 * w) * 8192 + 256 * rp), avo_ = (lane >> 3) * 8192 + (lane & 7) * 32;
#define W3_RD1(k, c) wfr[(k) % 3][c] = trd2(wc[c] + (k) * 4096, 2048)
#define W3_READ(k) do { _Pragma("unroll") for (int c = 0; c < 4; ++c) W3_RD1(k, c); SB(); } while (0)
#define H_WAIT(k, n) asm volatile("s_waitcnt vmcnt(%1)" : "+v"(hf[k]) : "i"(n) : "memory")
#define G3_M(k, c) do { a3[c] = mfma(wfr[(k) % 3][c], hf[k], a3[c]); SB(); } while (0)
#define FIN_PUT() do { _Pragma("unroll") for (int c = 0; c < 2; ++c) *(LAS f32x4*)(L + L_RED3 + w * 2048 + l16 + c * 1024) = a3[2 + c]; } while (0)
#define AT_LOAD() do { BLD(av0, avo_, aso, 0); BLD(av1, avo_, aso, 16); } while (0)
#define AT_STAGE() do { _Pragma("unroll 1") for (int sp = 0; sp < SPIN_CAP; ++sp) { \
        asm volatile("s_waitcnt vmcnt(0)" : "+v"(av0), "+v"(av1), "+v"(xr0), "+v"(xr1)::"memory"); \
        if (__all(((av0.y ^ tag) | (av0.w ^ tag) | (av1.y ^ tag) | (av1.w ^ tag)) == 0)) break; \
        AT_LOAD(); } \
      asm volatile("s_waitcnt vmcnt(0)" : "+v"(av0), "+v"(av1)::"memory"); \
      *(LAS v4u*)(L + L_AT + (8 * w + (lane >> 3)) * 144 + (lane & 7) * 16) = v4u{av0.x, av0.z, av1.x, av1.z}; } while (0)
    W3_READ(0); W3_READ(1);
    H_WAIT(0, 10); G3_M(0, 3); G3_M(0, 2); G3_M(0, 1); G3_M(0, 0); W3_READ(2); H_LOAD(11); H_LOAD(12);
    H_WAIT(1, 11); G3_M(1, 3); G3_M(1, 2); G3_M(1, 1); G3_M(1, 0); W3_READ(3); H_LOAD(13);
    H_WAIT(2, 11); G3_M(2, 3); G3_M(2, 2); G3_M(2, 1); G3_M(2, 0); W3_READ(4);
    H_WAIT(3, 10); G3_M(3, 3); G3_M(3, 2); G3_M(3, 1); G3_M(3, 0); W3_READ(5);
    H_WAIT(4, 9); G3_M(4, 3); G3_M(4, 2); G3_M(4, 1); G3_M(4, 0); W3_READ(6);
    H_WAIT(5, 8); G3_M(5, 3); G3_M(5, 2); G3_M(5, 1); G3_M(5, 0); W3_READ(7);
    H_WAIT(6, 7); G3_M(6, 3); G3_M(6, 2); G3_M(6, 1); G3_M(6, 0); W3_READ(8); bar(); STAMPX(11);
    H_WAIT(7, 6); G3_M(7, 3); G3_M(7, 2); G3_M(7, 1); G3_M(7, 0); W3_READ(9);
    H_WAIT(8, 5); G3_M(8, 3); G3_M(8, 2); G3_M(8, 1); G3_M(8, 0); W3_READ(10);
    H_WAIT(9, 4); G3_M(9, 3); G3_M(9, 2); G3_M(9, 1); G3_M(9, 0); W3_READ(11); AT_LOAD();
    H_WAIT(10, 5); G3_M(10, 3); G3_M(10, 2); G3_M(10, 1); G3_M(10, 0); W3_READ(12);
    H_WAIT(11, 4); G3_M(11, 3); G3_M(11, 2); G3_M(11, 1); G3_M(11, 0); W3_READ(13);
    H_WAIT(12, 3); G3_M(12, 3); G3_M(12, 2); G3_M(12, 1); G3_M(12, 0);
    H_WAIT(13, 2); G3_M(13, 3); G3_M(13, 2); G3_M(13, 1); G3_M(13, 0);
    FIN_PUT(); STAMPX(12); AT_STAGE(); STAMPX(13);
  }
  STAMP(14);
  bar();
#pragma unroll
  for (int c = 0; c < 2; ++c) {
    const f32x4 v = a3[c] + *(const LAS f32x4*)(L + L_RED3 + (w ^ 4) * 2048 + l16 + c * 1024);
    const v2u av = trd(L + L_AT + (32 * wm + 4 * g + wq) * 144 + (16 * wn + 4 * wp) * 2 + c * (16 * 144));
    const v2u xr = c ? xr1 : xr0;
    const v2u ov = {pk2(up2(xr.x, hm) + up2(av.x, hm) + v.xy), pk2(up2(xr.y, hm) + up2(av.y, hm) + v.zw)};
    asm volatile("global_store_dwordx2 %0, %1, %2 offset:%3" OPOL ::"v"(xo), "v"(ov), "s"(out), "i"(32 * c) : "memory");
  }
  STAMP(15);
  STAMP_OUT;
  asm volatile("fk_code_end:");
}

extern "C" void kernel_launch(void* const* d_in, const int* in_sizes, int n_in, void* d_out, int out_size, void* d_ws, size_t ws_size, hipStream_t stream) {
  (void)in_sizes; (void)n_in; (void)out_size; (void)ws_size;
  (void)hipFuncSetAttribute((const void*)fused_kernel, hipFuncAttributeMaxDynamicSharedMemorySize, LDS_TOTAL);
  fused_kernel<<<256, 512, LDS_TOTAL, stream>>>((const u16*)d_in[0], (const u16*)d_in[1], (const u16*)d_in[2], (const u16*)d_in[3], (const u16*)d_in[4],
                                               (const u16*)d_in[5], (const u16*)d_in[6], (const u16*)d_in[8], (const u16*)d_in[9], (const u16*)d_in[10],
                                               (const u16*)d_in[11], (u16*)d_out, (char*)d_ws);
}
```
